```python
import torch

def _softmax(x, axis=-1):
    return torch.softmax(x, dim=axis)
def _gelu(x):
    return torch.nn.functional.gelu(x, approximate="tanh")
def _silu(x):
    return torch.nn.functional.silu(x)
def _sigmoid(x):
    return torch.sigmoid(x)
def _layernorm(x, axis=-1, eps=1e-5):
    m = x.mean(dim=axis, keepdim=True)
    v = x.var(dim=axis, keepdim=True, unbiased=False)
    return (x - m) / torch.sqrt(v + eps)
def _scatter_add(tgt, idx, src, axis=0):
    return tgt.clone().index_add_(axis, idx, src)
def _segment_sum(src, seg, n):
    out = torch.zeros((n,) + src.shape[1:], dtype=src.dtype)
    return out.index_add_(0, seg, src)
def _pad(x, pw):
    flat = tuple(v for pair in reversed(pw) for v in pair)
    return torch.nn.functional.pad(x, flat)

kernel_name = 'sequential_preLN__gqa_block_cached__geglu_ffn__decode'

def setup_inputs():

    _w = (lambda a: (lambda t: t.to(torch.bfloat16) if t.is_floating_point() else t)(torch.as_tensor(a)))
    def _A_setup():
        B, Sq, D = 1, 1, 1024
        Hq, Hkv, Sk, Dh = 32, 4, 65536, 128
        assert Hq % Hkv == 0, (Hq, Hkv)
        return {
            "x": _w((lambda *s: torch.randn(*s, dtype=torch.bfloat16) * 0.05)(B, Sq, D)),
            "k_cache": _w((lambda *s: torch.randn(*s, dtype=torch.bfloat16) * 3.2)(B, Hkv, Sk, Dh)),
            "v_cache": _w((lambda *s: torch.randn(*s, dtype=torch.bfloat16) * 0.4)(B, Hkv, Sk, Dh)),
            "wq": _w((lambda *s: torch.randn(*s, dtype=torch.bfloat16) * 3.2)(D, Hq * Dh) * (D ** -0.5)),
            "wo": _w((lambda *s: torch.randn(*s, dtype=torch.bfloat16) * 0.4)(Hq * Dh, D) * ((Hq * Dh) ** -0.5)),
        }
    _A_in = _A_setup()

    def _B_setup():
        B, S, D, F = 1, 1, 1024, 3584
        return {
            "x": _w((lambda *s: torch.randn(*s, dtype=torch.bfloat16) * 0.05)(B, S, D)),
            "w_gate": _w((lambda *s: torch.randn(*s, dtype=torch.bfloat16) * 0.05)(D, F)),
            "w_up": _w((lambda *s: torch.randn(*s, dtype=torch.bfloat16) * 0.05)(D, F)),
            "w_down": _w((lambda *s: torch.randn(*s, dtype=torch.bfloat16) * 0.05)(F, D)),
        }
    _B_in = _B_setup()

    return {
        "A_x": _A_in["x"],
        "A_k_cache": _A_in["k_cache"],
        "A_v_cache": _A_in["v_cache"],
        "A_wq": _A_in["wq"],
        "A_wo": _A_in["wo"],
        "B_x": _B_in["x"],
        "B_w_gate": _B_in["w_gate"],
        "B_w_up": _B_in["w_up"],
        "B_w_down": _B_in["w_down"],
    }

def reference(A_x, A_k_cache, A_v_cache, A_wq, A_wo, B_x, B_w_gate, B_w_up, B_w_down):
    def _A_ref(x, k_cache, v_cache, wq, wo):
        B, Sq, D = x.shape
        Hq, Dh = 32, 128
        Hkv = k_cache.shape[1]
        rep = Hq // Hkv
        q = (lambda x, *d: x.permute(*d))((x @ wq).reshape(B, Sq, Hq, Dh), 0, 2, 1, 3)
        k = (lambda x, n, axis: torch.repeat_interleave(x, n, dim=axis))(k_cache, rep, axis=1)
        v = (lambda x, n, axis: torch.repeat_interleave(x, n, dim=axis))(v_cache, rep, axis=1)
        scores = torch.einsum("bhqd,bhkd->bhqk", q, k) * (Dh ** -0.5)
        p = _softmax(scores, axis=-1)
        o = torch.einsum("bhqk,bhkd->bhqd", p, v)
        return (lambda x, *d: x.permute(*d))(o, 0, 2, 1, 3).reshape(B, Sq, Hq * Dh) @ wo

    def _B_ref(x, w_gate, w_up, w_down):
        return (_gelu(x @ w_gate) * (x @ w_up)) @ w_down

    x = A_x
    h = x + _A_ref(_layernorm(x, axis=-1), A_k_cache, A_v_cache, A_wq, A_wo)
    h2 = h + _B_ref(_layernorm(h, axis=-1), B_w_gate, B_w_up, B_w_down)
    return h2
```

```cpp
#include <hip/hip_runtime.h>
#include <hip/hip_bf16.h>
#include <stdint.h>

typedef unsigned v4u __attribute__((ext_vector_type(4)));
typedef float f32x4 __attribute__((ext_vector_type(4)));
typedef short bf16x8 __attribute__((ext_vector_type(8)));
typedef unsigned short u16;

#define KV_NT 0

constexpr int D = 1024, HQ = 32, HKV = 4, SK = 65536, DH = 128, F = 3584;
constexpr int NSPLIT = 64;

__device__ __forceinline__ float bf2f(unsigned b) { return __builtin_bit_cast(float, b << 16); }
__device__ __forceinline__ float bflo(unsigned w) { return __builtin_bit_cast(float, w << 16); }
__device__ __forceinline__ float bfhi(unsigned w) { return __builtin_bit_cast(float, w & 0xffff0000u); }
__device__ __forceinline__ unsigned cvtpk(float lo, float hi) {
  unsigned r; asm("v_cvt_pk_bf16_f32 %0, %1, %2" : "=v"(r) : "v"(lo), "v"(hi)); return r;
}
__device__ __forceinline__ float rbf(float x) { return bflo(cvtpk(x, x)); }
__device__ __forceinline__ v4u ldnt(const v4u* p) { return KV_NT ? __builtin_nontemporal_load(p) : *p; }
__device__ __forceinline__ f32x4 mfma(v4u a, v4u b, f32x4 c) {
  return __builtin_amdgcn_mfma_f32_16x16x32_bf16(__builtin_bit_cast(bf16x8, a), __builtin_bit_cast(bf16x8, b), c, 0, 0, 0);
}
__device__ __forceinline__ float block_sum(float v, float* red, int nw) {
  for (int o = 32; o > 0; o >>= 1) v += __shfl_xor(v, o);
  __syncthreads();
  if ((threadIdx.x & 63) == 0) red[threadIdx.x >> 6] = v;
  __syncthreads();
  float s = 0.f;
  for (int i = 0; i < nw; ++i) s += red[i];
  return s;
}

template <int K, int N>
__device__ __forceinline__ void gemv16(const u16* W, int col0, const float* xs, float* red  , float* out16) {
  const int t = threadIdx.x, half = t & 1, r0 = t >> 1;
  float acc[8];
#pragma unroll
  for (int j = 0; j < 8; ++j) acc[j] = 0.f;
#pragma unroll
  for (int i = 0; i < K / 256; ++i) {
    const int r = r0 + 256 * i;
    const v4u w = *(const v4u*)(W + (size_t)r * N + col0 + 8 * half);
    const float xv = xs[r];
#pragma unroll
    for (int j = 0; j < 4; ++j) { acc[2 * j] += xv * bflo(w[j]); acc[2 * j + 1] += xv * bfhi(w[j]); }
  }
#pragma unroll
  for (int j = 0; j < 8; ++j)
    for (int o = 32; o > 1; o >>= 1) acc[j] += __shfl_xor(acc[j], o);
  __syncthreads();
  if ((t & 63) < 2)
    for (int j = 0; j < 8; ++j) red[(t >> 6) * 16 + half * 8 + j] = acc[j];
  __syncthreads();
  if (t < 16) { float s = 0.f; for (int w = 0; w < 8; ++w) s += red[w * 16 + t]; out16[t] = s; }
  __syncthreads();
}

__device__ __forceinline__ void layernorm_lds(float* xs, float* red) {
  const int t = threadIdx.x;
  const float a = xs[t], b = xs[t + 512];
  const float mean = block_sum(a + b, red, 8) * (1.f / D);
  const float var = block_sum((a - mean) * (a - mean) + (b - mean) * (b - mean), red, 8) * (1.f / D);
  const float rs = 1.f / sqrtf(var + 1e-5f);
  __syncthreads();
  xs[t] = rbf((a - mean) * rs); xs[t + 512] = rbf((b - mean) * rs);
  __syncthreads();
}

__global__ void __launch_bounds__(512) k_q(const u16* x, const u16* wq, u16* q) {
  __shared__ float xs[D]; __shared__ float red[128]; __shared__ float o16[16];
  const int t = threadIdx.x;
  xs[t] = bf2f(x[t]); xs[t + 512] = bf2f(x[t + 512]);
  __syncthreads();
  layernorm_lds(xs, red);
  const int b = blockIdx.x, cb = (b & 7) * 32 + (b >> 3);
  gemv16<D, HQ * DH>(wq, cb * 16, xs, red, o16);
  if (t < 16) q[cb * 16 + t] = (u16)cvtpk(o16[t], 0.f);
}

__global__ void __launch_bounds__(512) k_attn(const u16* q, const u16* kc, const u16* vc, float* pml, float* po) {
  __shared__ float smax[8][16]; __shared__ float ssum[8][16];
  __shared__ __attribute__((aligned(16))) float so[8][1024];
  const int t = threadIdx.x, lane = t & 63, w = __builtin_amdgcn_readfirstlane(t >> 6);
  const int g = blockIdx.x >> 6, s = blockIdx.x & 63;
  const int n = lane & 15, qg = lane >> 4;
  const size_t key0 = (size_t)g * SK + s * 1024 + w * 128;
  const v4u* kp = (const v4u*)(kc + (key0 + 4 * (n & 3) + (n >> 2)) * DH + 8 * qg);
  const v4u* vp = (const v4u*)(vc + key0 * DH) + lane;
  v4u buf[32];
  v4u qf[4];
#pragma unroll
  for (int u = 0; u < 4; ++u) qf[u] = *(const v4u*)(q + (g * 8 + (n & 7)) * DH + 32 * u + 8 * qg);
#pragma unroll
  for (int i = 0; i < 32; ++i) buf[i] = ldnt(kp + (i >> 2) * 16 * 16 + (i & 3) * 4);
  __builtin_amdgcn_sched_barrier(0);
  f32x4 S[8];
#pragma unroll
  for (int tl = 0; tl < 8; ++tl) {
    f32x4 a = {0.f, 0.f, 0.f, 0.f};
#pragma unroll
    for (int u = 0; u < 4; ++u) a = mfma(buf[tl * 4 + u], qf[u], a);
    S[tl] = a;
#pragma unroll
    for (int u = 0; u < 4; ++u) buf[tl * 4 + u] = ldnt(vp + (tl * 4 + u) * 64);
    __builtin_amdgcn_sched_barrier(0);
  }
  constexpr float C2 = 0.088388347648318440f * 1.4426950408889634f;
  float m = -INFINITY;
#pragma unroll
  for (int tl = 0; tl < 8; ++tl)
#pragma unroll
    for (int r = 0; r < 4; ++r) m = fmaxf(m, S[tl][r]);
  m = fmaxf(m, __shfl_xor(m, 16)); m = fmaxf(m, __shfl_xor(m, 32));
  if (lane < 16) smax[w][lane] = m;
  __syncthreads();
#pragma unroll
  for (int i = 0; i < 8; ++i) m = fmaxf(m, smax[i][n]);
  const float mc = m * C2;
  float l = 0.f;
  v4u P[4];
#pragma unroll
  for (int tp = 0; tp < 4; ++tp) {
    float p[8];
#pragma unroll
    for (int j = 0; j < 8; ++j) { p[j] = __builtin_amdgcn_exp2f(S[2 * tp + (j >> 2)][j & 3] * C2 - mc); l += p[j]; }
#pragma unroll
    for (int j = 0; j < 4; ++j) P[tp][j] = cvtpk(p[2 * j], p[2 * j + 1]);
  }
  l += __shfl_xor(l, 16); l += __shfl_xor(l, 32);
  if (lane < 16) ssum[w][lane] = l;
  f32x4 O[8];
#pragma unroll
  for (int e = 0; e < 8; ++e) O[e] = f32x4{0.f, 0.f, 0.f, 0.f};
#pragma unroll
  for (int tp = 0; tp < 4; ++tp) {
#pragma unroll
    for (int e = 0; e < 8; ++e) {
      v4u a;
#pragma unroll
      for (int j = 0; j < 4; ++j) {
        const unsigned lo = buf[tp * 8 + 2 * j][e >> 1], hi = buf[tp * 8 + 2 * j + 1][e >> 1];
        a[j] = __builtin_amdgcn_perm(hi, lo, (e & 1) ? 0x07060302u : 0x05040100u);
      }
      O[e] = mfma(a, P[tp], O[e]);
    }
  }
  if (n < 8) {
#pragma unroll
    for (int r = 0; r < 4; ++r) {
      float* dst = &so[w][n * 128 + 8 * (4 * qg + r)];
      *(f32x4*)dst = f32x4{O[0][r], O[1][r], O[2][r], O[3][r]};
      *(f32x4*)(dst + 4) = f32x4{O[4][r], O[5][r], O[6][r], O[7][r]};
    }
  }
  __syncthreads();
  float a0 = 0.f, a1 = 0.f;
#pragma unroll
  for (int i = 0; i < 8; ++i) { a0 += so[i][2 * t]; a1 += so[i][2 * t + 1]; }
  *(float2*)(po + (size_t)blockIdx.x * 1024 + 2 * t) = make_float2(a0, a1);
  if (t < 8) {
    float L = 0.f;
    for (int i = 0; i < 8; ++i) L += ssum[i][t];
    pml[(blockIdx.x * 8 + t) * 2] = mc; pml[(blockIdx.x * 8 + t) * 2 + 1] = L;
  }
}

__global__ void __launch_bounds__(128) k_comb(const float* pml, const float* po, float* o) {
  const int h = blockIdx.x, g = h >> 3, hh = h & 7, t = threadIdx.x;
  float M = -INFINITY;
  for (int s = 0; s < NSPLIT; ++s) M = fmaxf(M, pml[((g * 64 + s) * 8 + hh) * 2]);
  float L = 0.f, a = 0.f;
  for (int s = 0; s < NSPLIT; ++s) {
    const float wt = __builtin_amdgcn_exp2f(pml[((g * 64 + s) * 8 + hh) * 2] - M);
    L += wt * pml[((g * 64 + s) * 8 + hh) * 2 + 1];
    a += wt * po[(size_t)(g * 64 + s) * 1024 + hh * 128 + t];
  }
  o[h * 128 + t] = rbf(a / L);
}

__global__ void __launch_bounds__(512) k_wo(const u16* x, const float* o, const u16* wo, float* h) {
  __shared__ float xs[4096]; __shared__ float red[128]; __shared__ float o16[16];
  const int t = threadIdx.x;
  for (int i = t; i < 4096; i += 512) xs[i] = o[i];
  __syncthreads();
  const int b = blockIdx.x, cb = (b & 7) * 8 + (b >> 3);
  gemv16<4096, D>(wo, cb * 16, xs, red, o16);
  if (t < 16) h[cb * 16 + t] = rbf(bf2f(x[cb * 16 + t]) + rbf(o16[t]));
}

__global__ void __launch_bounds__(512) k_gu(const float* h, const u16* wg, const u16* wu, float* act) {
  __shared__ float xs[D]; __shared__ float red[128]; __shared__ float g16[16]; __shared__ float u16v[16];
  const int t = threadIdx.x;
  xs[t] = h[t]; xs[t + 512] = h[t + 512];
  __syncthreads();
  layernorm_lds(xs, red);
  const int b = blockIdx.x, cb = (b & 7) * 28 + (b >> 3);
  gemv16<D, F>(wg, cb * 16, xs, red, g16);
  gemv16<D, F>(wu, cb * 16, xs, red, u16v);
  if (t < 16) {
    const float gg = rbf(g16[t]), uu = rbf(u16v[t]);
    const float z = 0.7978845608028654f * (gg + 0.044715f * gg * gg * gg);
    const float ge = rbf(0.5f * gg * (1.f + tanhf(z)));
    act[cb * 16 + t] = rbf(ge * uu);
  }
}

__global__ void __launch_bounds__(512) k_down(const float* h, const float* act, const u16* wd, u16* out) {
  __shared__ float xs[F]; __shared__ float red[128]; __shared__ float o16[16];
  const int t = threadIdx.x;
  for (int i = t; i < F; i += 512) xs[i] = act[i];
  __syncthreads();
  const int b = blockIdx.x, cb = (b & 7) * 8 + (b >> 3);
  gemv16<F, D>(wd, cb * 16, xs, red, o16);
  if (t < 16) out[cb * 16 + t] = (u16)cvtpk(h[cb * 16 + t] + rbf(o16[t]), 0.f);
}

extern "C" void kernel_launch(void* const* d_in, const int* in_sizes, int n_in, void* d_out, int out_size, void* d_ws,
                              size_t ws_size, hipStream_t stream) {
  const u16* x = (const u16*)d_in[0];
  const u16* kc = (const u16*)d_in[1];
  const u16* vc = (const u16*)d_in[2];
  const u16* wq = (const u16*)d_in[3];
  const u16* wo = (const u16*)d_in[4];
  const u16* wg = (const u16*)d_in[6];
  const u16* wu = (const u16*)d_in[7];
  const u16* wd = (const u16*)d_in[8];
  char* ws = (char*)d_ws;
  u16* q = (u16*)ws;
  float* pml = (float*)(ws + 16384);
  float* o = (float*)(ws + 32768);
  float* h = (float*)(ws + 49152);
  float* act = (float*)(ws + 65536);
  float* po = (float*)(ws + 131072);
  k_q<<<256, 512, 0, stream>>>(x, wq, q);
  k_attn<<<256, 512, 0, stream>>>(q, kc, vc, pml, po);
  k_comb<<<32, 128, 0, stream>>>(pml, po, o);
  k_wo<<<64, 512, 0, stream>>>(x, o, wo, h);
  k_gu<<<224, 512, 0, stream>>>(h, wg, wu, act);
  k_down<<<64, 512, 0, stream>>>(h, act, wd, (u16*)d_out);
}
```

```cpp
#define PACE 2800
#define C1 150
#define C2 100
#define C3 110
#define C4 92
#define EM 0
#define ES 0
#include <hip/hip_runtime.h>
#include <hip/hip_bf16.h>
#include <stdint.h>

#define I0 0
#define NF 3
#define BA 0
#define TIMED 1
#define ABL 0
#define MAXO 40
#define NB 5
#define N1A 3
#define N1B 0
#define N1C 0
#define N2A 4
#define N2B 0
#define N2C 0
#define N3A 3
#define N3B 0
#define TA 14
#define TLBW 1
#define WARM 1
#define FILLS 1
#define TB 28
#define RM 8
#define RB1 28
#define RB 32
#define OUTSC 0
#define CODEPF 1
#define STAMPS 0

#define TUNEARG
#define TUNEVAL
typedef unsigned v4u __attribute__((ext_vector_type(4)));
typedef unsigned v2u __attribute__((ext_vector_type(2)));
typedef float f32x4 __attribute__((ext_vector_type(4)));
typedef float f32x2 __attribute__((ext_vector_type(2)));
typedef short bf16x8 __attribute__((ext_vector_type(8)));
typedef short s16x4 __attribute__((ext_vector_type(4)));
typedef __bf16 bf16x2 __attribute__((ext_vector_type(2)));
typedef unsigned short u16;
typedef __amdgpu_buffer_rsrc_t rsrc_t;
#define LAS __attribute__((address_space(3)))

constexpr int D = 1024, SK = 65536, DH = 128, F = 3584;
constexpr int SC1 = 16;
constexpr unsigned SENT = 0x7FF5A5A5u;
constexpr int SPIN_CAP = 1 << 17;
constexpr float SCALE = 0.088388347648318440f, LOG2E = 1.4426950408889634f;

constexpr int WS_E = 0, WS_FLAG = 4096, WS_T0 = 8192, WS_QP = 16384, WS_S = 131072;
constexpr int S_H1 = WS_S, S_H2 = S_H1 + (1 << 20), S_H3 = S_H2 + (256 << 10), S_H4 = S_H3 + 16 * 2 * F * 4;
constexpr int S_ML = S_H4 + (256 << 10), S_MU = S_ML + 16384, S_SS = S_MU + 256 * 128, S_RS = S_SS + 128 * 128, S_END = S_RS + 256 * 256;
static_assert(256 * (8192 + 256) >= S_END - WS_S, "clear shares cover the sentinel region");
constexpr int WS_BYTES = WS_S + 256 * (8192 + 256);
constexpr int OOB = WS_BYTES;
constexpr int WS_STAMP = 4 << 20;

constexpr int BUF = 32768;
constexpr int L_P = 4 * BUF, L_QRED = L_P + 16384, L_QS = L_QRED + 2048, L_MAX = L_QS + 8 * 272, L_SUM = L_MAX + 256;
constexpr int L_ML = L_SUM + 256, L_RS = L_ML + 512, L_R1 = L_RS + 1024, L_R2 = L_R1 + 1024, L_R3 = L_R2 + 1024;
constexpr int L_MU = L_R3 + 1024, L_X = L_MU + 16, L_E = L_X + 2048, L_JUNK = L_E + 256, L_T = L_JUNK + 256, L_LST = L_T + 16, L_BYTES = L_LST + (STAMPS ? 512 : 0);
constexpr int B_WO = (9 % 3) * BUF, B_GUA = (10 % 3) * BUF, B_GUB = 3 * BUF, B_DN = (19 % 3) * BUF;
constexpr int cmin(int a, int b) { return a < b ? a : b; }
constexpr int NG = 15;
constexpr int G1A = cmin(NB + N1A, NG), G1B = cmin(G1A + N1B, NG), G1END = cmin(G1B + N1C, NG);
constexpr int G2A = cmin(G1END + N2A, NG), G2B = cmin(G2A + N2B, NG), G2END = cmin(G2B + N2C, NG);
constexpr int G3A = cmin(G2END + N3A, NG), G3B = cmin(G3A + N3B, NG);
constexpr bool NEED_G0 = G1END < 8, NEED_H0 = G2END < NG;
static_assert(NB <= 8 && G2END >= 8, "late-weight schedule");
constexpr int npieces(int lo, int hi) { int s = 0; if (ABL & 16) return 0; for (int i = lo; i < hi; ++i) s += i < 8 ? 3 : 4; return s; }

__device__ __forceinline__ float bflo(unsigned w) { return __builtin_bit_cast(float, w << 16); }
__device__ __forceinline__ float bfhi(unsigned w) { return __builtin_bit_cast(float, w & 0xffff0000u); }
__device__ __forceinline__ unsigned cvtpk(float lo, float hi) {
  f32x2 v = {lo, hi};
  return __builtin_bit_cast(unsigned, __builtin_convertvector(v, bf16x2));
}
__device__ __forceinline__ float rbf(float x) { return bflo(cvtpk(x, x)); }
__device__ __forceinline__ float asf(unsigned u) { return __builtin_bit_cast(float, u); }
__device__ __forceinline__ unsigned asu(float f) { return __builtin_bit_cast(unsigned, f); }
__device__ __forceinline__ f32x4 asf4(v4u u) { return __builtin_bit_cast(f32x4, u); }
__device__ __forceinline__ float dot2(unsigned a, unsigned b, float c) {
  return __builtin_amdgcn_fdot2_f32_bf16(__builtin_bit_cast(bf16x2, a), __builtin_bit_cast(bf16x2, b), c, false);
}
__device__ __forceinline__ f32x4 mfma(v4u a, v4u b, f32x4 c) {
  return __builtin_amdgcn_mfma_f32_16x16x32_bf16(__builtin_bit_cast(bf16x8, a), __builtin_bit_cast(bf16x8, b), c, 0, 0, 0);
}
template <int CTRL> __device__ __forceinline__ float dpp(float x) {
  return __builtin_bit_cast(float, __builtin_amdgcn_mov_dpp(__builtin_bit_cast(int, x), CTRL, 0xf, 0xf, true));
}
constexpr int X1 = 0xB1, X2 = 0x4E, HM = 0x141, R8 = 0x128;
__device__ __forceinline__ float tr32(float a, float b) {
  auto s = __builtin_amdgcn_permlane32_swap(asu(a), asu(b), false, false);
  return asf(s[0]) + asf(s[1]);
}
__device__ __forceinline__ float tr16(float a, float b) {
  auto s = __builtin_amdgcn_permlane16_swap(asu(a), asu(b), false, false);
  return asf(s[0]) + asf(s[1]);
}
__device__ __forceinline__ float sum8(float x) { x += dpp<X1>(x); x += dpp<X2>(x); x += dpp<HM>(x); return x; }
__device__ __forceinline__ float sum16(float x) { x = sum8(x); return x + dpp<R8>(x); }
__device__ __forceinline__ float sum45(float x) { x = tr16(x, x); return tr32(x, x); }
__device__ __forceinline__ float sum64(float x) { return sum45(sum16(x)); }
__device__ __forceinline__ float max45(float x) {
  auto s = __builtin_amdgcn_permlane16_swap(asu(x), asu(x), false, false);
  x = fmaxf(asf(s[0]), asf(s[1]));
  auto t = __builtin_amdgcn_permlane32_swap(asu(x), asu(x), false, false);
  return fmaxf(asf(t[0]), asf(t[1]));
}
__device__ __forceinline__ float red4(f32x4 v) { return tr16(tr32(v[0], v[2]), tr32(v[1], v[3])); }
__device__ __forceinline__ void pack64(float x, unsigned& in0, unsigned& in1) {
  auto s = __builtin_amdgcn_permlane16_swap(asu(x), asu(x), false, false);
  const unsigned p = cvtpk(asf(s[0]), asf(s[1]));
  auto t = __builtin_amdgcn_permlane32_swap(p, p, false, false);
  in0 = t[0]; in1 = t[1];
}
__device__ __forceinline__ rsrc_t mk(const void* p, int bytes) { return __builtin_amdgcn_make_buffer_rsrc((void*)p, 0, bytes, 0x00020000); }
__device__ __forceinline__ v4u mksrd(const void* p, int bytes) {
  const unsigned long long a = (unsigned long long)p;
  return v4u{(unsigned)a, (unsigned)(a >> 32) & 0xffffu, (unsigned)bytes, 0x00020000u};
}
__device__ __forceinline__ v4u ldc4(rsrc_t r, int vo) { return __builtin_amdgcn_raw_buffer_load_b128(r, vo, 0, SC1); }
__device__ __forceinline__ v2u ldc2(rsrc_t r, int vo) { return __builtin_amdgcn_raw_buffer_load_b64(r, vo, 0, SC1); }
__device__ __forceinline__ unsigned ldc1(rsrc_t r, int vo) { return __builtin_amdgcn_raw_buffer_load_b32(r, vo, 0, SC1); }
__device__ __forceinline__ void stc4(rsrc_t r, int vo, v4u v) { __builtin_amdgcn_raw_buffer_store_b128(v, r, vo, 0, SC1); }
__device__ __forceinline__ void stc2(rsrc_t r, int vo, v2u v) { __builtin_amdgcn_raw_buffer_store_b64(v, r, vo, 0, SC1); }
__device__ __forceinline__ void stc1(rsrc_t r, int vo, unsigned v) { __builtin_amdgcn_raw_buffer_store_b32(v, r, vo, 0, SC1); }
__device__ __forceinline__ v2u trd(LAS unsigned char* p) {
  return __builtin_bit_cast(v2u, __builtin_amdgcn_ds_read_tr16_b64_v4i16((LAS s16x4*)p));
}
#define FENCE() do { asm volatile("" ::: "memory"); __builtin_amdgcn_sched_barrier(0); } while (0)
#define PIN(x) asm volatile("" : "+v"(x))
template <int N> __device__ __forceinline__ void waitvm() { asm volatile("s_waitcnt vmcnt(%0)" ::"i"(N) : "memory"); }
__device__ __forceinline__ void bar() {
  __builtin_amdgcn_sched_barrier(0);
  asm volatile("s_waitcnt lgkmcnt(0)\n\ts_barrier" ::: "memory");
  __builtin_amdgcn_sched_barrier(0);
}
__device__ __forceinline__ void pace_wait(unsigned due) {
  asm volatile("1:\n\ts_memrealtime vcc\n\ts_waitcnt lgkmcnt(0)\n\ts_sub_u32 vcc_lo, vcc_lo, %0\n\ts_cmp_lt_i32 vcc_lo, 0\n\t"
               "s_cbranch_scc0 2f\n\ts_sleep 1\n\ts_branch 1b\n2:" ::"s"(due) : "vcc", "scc", "memory");
}
__device__ __forceinline__ void time_wait(unsigned due) {
  asm volatile("1:\n\ts_memrealtime vcc\n\ts_waitcnt lgkmcnt(0)\n\ts_sub_u32 vcc_lo, vcc_lo, %0\n\ts_cmp_lt_i32 vcc_lo, 0\n\t"
               "s_cbranch_scc1 1b" ::"s"(due) : "vcc", "scc", "memory");
}
__device__ __forceinline__ unsigned rebase(unsigned due, int lead, int late) {
  const unsigned now = (unsigned)__builtin_amdgcn_s_memrealtime();
  return (int)(now + lead + RM - due) > 0 ? now + lead + late : due;
}
#define DMA0(v) "s_mov_b32 m0, %5\n\ts_mov_b32 %0, %4\n\tbuffer_load_dwordx4 %" #v ", %3, %0 offen nt lds\n\t"
#define DMA1(v) "s_add_u32 m0, m0, %7\n\ts_add_u32 %0, %0, %6\n\tbuffer_load_dwordx4 %" #v ", %3, %0 offen nt lds\n\t"
template <int N, int GSTEP, int LSTEP> __device__ __forceinline__ void dma(v4u srd, int va, int vb, int so, int lds) {
  int tmp;
  if constexpr (N == 8)
    asm volatile(DMA0(1) DMA1(1) DMA1(2) DMA1(2) DMA1(1) DMA1(1) DMA1(2) DMA1(2)
                 : "=&s"(tmp) : "v"(va), "v"(vb), "s"(srd), "s"(so), "s"(lds), "i"(GSTEP), "i"(LSTEP) : "scc", "memory");
  else if constexpr (N == 4)
    asm volatile(DMA0(1) DMA1(1) DMA1(2) DMA1(2)
                 : "=&s"(tmp) : "v"(va), "v"(vb), "s"(srd), "s"(so), "s"(lds), "i"(GSTEP), "i"(LSTEP) : "scc", "memory");
  else
    asm volatile(DMA0(1) DMA1(1) DMA1(2)
                 : "=&s"(tmp) : "v"(va), "v"(vb), "s"(srd), "s"(so), "s"(lds), "i"(GSTEP), "i"(LSTEP) : "scc", "memory");
}
__device__ __forceinline__ void dma_x4(v4u srd, int vo, int lds) {
  asm volatile("s_mov_b32 m0, %2\n\ts_nop 0\n\tbuffer_load_dwordx4 %0, %1, 0 offen lds" ::"v"(vo), "s"(srd), "s"(lds) : "memory");
}
__device__ __forceinline__ void dma_x1(v4u srd, int vo, int lds) {
  asm volatile("s_mov_b32 m0, %2\n\ts_nop 0\n\tbuffer_load_dword %0, %1, 0 offen lds" ::"v"(vo), "s"(srd), "s"(lds) : "memory");
}
__device__ __forceinline__ bool miss4(v4u v) { return v[0] == SENT || v[1] == SENT || v[2] == SENT || v[3] == SENT; }
template <int N> __device__ __forceinline__ void settle4(v4u (&v)[N], rsrc_t r, int vo, int step) {
#pragma nounroll
  for (int sp = 0; sp < SPIN_CAP; ++sp) {
    bool m = false;
#pragma unroll
    for (int i = 0; i < N; ++i) m |= miss4(v[i]);
    if (!__any(m)) break;
    asm volatile("" ::: "memory");
    if (m) {
#pragma unroll
      for (int i = 0; i < N; ++i) v[i] = ldc4(r, vo + i * step);
    }
  }
}
template <int N> __device__ __forceinline__ void settle1(unsigned (&v)[N], rsrc_t r, int vo, int step) {
#pragma nounroll
  for (int sp = 0; sp < SPIN_CAP; ++sp) {
    bool m = false;
#pragma unroll
    for (int i = 0; i < N; ++i) m |= v[i] == SENT;
    if (!__any(m)) break;
    asm volatile("" ::: "memory");
    if (m) {
#pragma unroll
      for (int i = 0; i < N; ++i) v[i] = ldc1(r, vo + i * step);
    }
  }
}
#define DOTL(a, j, w) "v_dot2c_f32_bf16_dpp %" #a ", %4, %" #w " row_newbcast:" #j " row_mask:0xf bank_mask:0xf\n\t"
__device__ __forceinline__ void gv16(float (&a)[4], unsigned in, const v2u* w) {
  asm volatile("s_nop 1\n\t" DOTL(0, 0, 5) DOTL(1, 1, 6) DOTL(2, 2, 7) DOTL(3, 3, 8) DOTL(0, 4, 9) DOTL(1, 5, 10) DOTL(2, 6, 11) DOTL(3, 7, 12)
               DOTL(0, 8, 13) DOTL(1, 9, 14) DOTL(2, 10, 15) DOTL(3, 11, 16) DOTL(0, 12, 17) DOTL(1, 13, 18) DOTL(2, 14, 19) DOTL(3, 15, 20)
               : "+v"(a[0]), "+v"(a[1]), "+v"(a[2]), "+v"(a[3])
               : "v"(in), "v"(w[0][0]), "v"(w[0][1]), "v"(w[1][0]), "v"(w[1][1]), "v"(w[2][0]), "v"(w[2][1]), "v"(w[3][0]), "v"(w[3][1]),
                 "v"(w[4][0]), "v"(w[4][1]), "v"(w[5][0]), "v"(w[5][1]), "v"(w[6][0]), "v"(w[6][1]), "v"(w[7][0]), "v"(w[7][1]));
}
#define GVFIN "s_nop 0\n\tv_add_f32 %0, %0, %1\n\ts_nop 0\n\tv_add_f32 %2, %2, %3\n\tv_add_f32 %0, %0, %2"
__device__ __forceinline__ float gv16f(float (&a)[4], unsigned in, const v2u* w) {
  asm volatile(DOTL(0, 0, 5) DOTL(1, 1, 6) DOTL(2, 2, 7) DOTL(3, 3, 8) DOTL(0, 4, 9) DOTL(1, 5, 10) DOTL(2, 6, 11) DOTL(3, 7, 12)
               DOTL(0, 8, 13) DOTL(1, 9, 14) DOTL(2, 10, 15) DOTL(3, 11, 16) DOTL(0, 12, 17) DOTL(1, 13, 18) DOTL(2, 14, 19) DOTL(3, 15, 20) GVFIN
               : "+v"(a[0]), "+v"(a[1]), "+v"(a[2]), "+v"(a[3])
               : "v"(in), "v"(w[0][0]), "v"(w[0][1]), "v"(w[1][0]), "v"(w[1][1]), "v"(w[2][0]), "v"(w[2][1]), "v"(w[3][0]), "v"(w[3][1]),
                 "v"(w[4][0]), "v"(w[4][1]), "v"(w[5][0]), "v"(w[5][1]), "v"(w[6][0]), "v"(w[6][1]), "v"(w[7][0]), "v"(w[7][1]));
  return a[0];
}
__device__ __forceinline__ float gv12f(float (&a)[4], unsigned in, const v2u* w) {
  asm volatile(DOTL(0, 0, 5) DOTL(1, 1, 6) DOTL(2, 2, 7) DOTL(3, 3, 8) DOTL(0, 4, 9) DOTL(1, 5, 10) DOTL(2, 6, 11) DOTL(3, 7, 12)
               DOTL(0, 8, 13) DOTL(1, 9, 14) DOTL(2, 10, 15) DOTL(3, 11, 16) GVFIN
               : "+v"(a[0]), "+v"(a[1]), "+v"(a[2]), "+v"(a[3])
               : "v"(in), "v"(w[0][0]), "v"(w[0][1]), "v"(w[1][0]), "v"(w[1][1]), "v"(w[2][0]), "v"(w[2][1]), "v"(w[3][0]), "v"(w[3][1]),
                 "v"(w[4][0]), "v"(w[4][1]), "v"(w[5][0]), "v"(w[5][1]));
  return a[0];
}
#define PAD(ch, i, j) asm volatile(".Lpad" #ch "_" #i ": s_cbranch_execz .Lpad" #ch "_" #j)
#define PADRUN(ch) asm volatile("s_mov_b64 vcc, exec\n\ts_mov_b64 exec, 0\n\ts_branch .Lpad" #ch "_0\n.Lpad" #ch "_e: s_mov_b64 exec, vcc" ::: "vcc")
#define FILL(d) do { __builtin_amdgcn_sched_barrier(0); asm volatile("v_mov_b32 %0, %0\n\ts_nop 1\n\tv_mov_b32 %0, %0\n\ts_nop 1\n\tv_mov_b32 %0, %0\n\ts_nop 1\n\tv_mov_b32 %0, %0" : "+v"(d)); __builtin_amdgcn_sched_barrier(0); } while (0)
#define ZERO4(a) float a[4] = {0.f, 0.f, 0.f, 0.f}; PIN(a[0]); PIN(a[1]); PIN(a[2]); PIN(a[3])

#define STAMP(i) do {} while (0)
#define STAMP_OUT() do {} while (0)
#define LBAR(i) bar()
#define LST(i) do {} while (0)
#define RETRY(i) do {} while (0)

__global__ void __launch_bounds__(576, 3)
fused_decode_block(unsigned char* ws, const u16* wq, const u16* x, const u16* kc, const u16* vc, const u16* wo, const u16* wg, const u16* wu,
                   const u16* wd, u16* out TUNEARG) {
  __shared__ __attribute__((aligned(16))) unsigned char lds_[L_BYTES];
  LAS unsigned char* L = (LAS unsigned char*)lds_;
  const int lds0 = (int)(unsigned)(size_t)L;
  unsigned long long pc; int codelen;
  asm volatile("s_getpc_b64 %0\n.Lpf_here:\n\ts_mov_b32 %1, .Lpf_end-.Lpf_here+256" : "=s"(pc), "=s"(codelen));
  dma_x1(mksrd((const void*)pc, codelen), threadIdx.x * 128, lds0 + L_JUNK);
  const int w = __builtin_amdgcn_readfirstlane(threadIdx.x >> 6), b = blockIdx.x;
  const int g = b >> 6, sp = b & 63, vb = (b & 7) * 32 + (b >> 3);
  const int r0 = sp >> 4, c0 = sp & 15;
  const int r1 = vb >> 2, c1 = vb & 3;
  const int g1 = r1 >> 4, rq = r1 & 15;
  const int r2 = vb >> 4, c2 = vb & 15;
  constexpr int STEP = PACE * 256 / 100, HEAD = (I0) * STEP;
  const int LASTV = (75 * STEP - HEAD + NB * (STEP * 3 / 8)) >> 8;

  if (w == 0) {
    __builtin_amdgcn_s_setprio(3);
    const int lane = threadIdx.x, lg = lane >> 3, lc = lane & 7, q4 = lane >> 4, n = lane & 15;
    const int wsw = (lc ^ (4 * ((lg >> 1) & 1))) * 16;
    const v4u sQ = mksrd(wq, D * 4096 * 2);
    const int qvo = lg * 8192 + wsw, qso = 256 * r0 * 8192 + (1024 * g + 64 * c0) * 2;
    dma<8, 65536, 1024>(sQ, qvo, qvo, qso, lds0);
    const unsigned tstart = (unsigned)__builtin_amdgcn_s_memrealtime();
    *(LAS unsigned*)(L + L_T) = tstart;
    int accF = STEP - HEAD, step = STEP;
    unsigned tcommon = tstart;
    auto tick = [&](int eighths) {
      if (PACE) { pace_wait(tstart + (accF >> 8)); accF += eighths == 8 ? step : STEP * eighths / 8; }
      waitvm<MAXO - 8>();
    };
    LST(0);
    {
      const v4u sX = mksrd(x, D * 2);
      dma_x4(sX, lane * 16, lds0 + L_X);
      dma_x4(sX, lane * 16 + 1024, lds0 + L_X + 1024);
      dma_x1(mksrd(ws, 8), lane * 4, lds0 + L_E);
#pragma unroll 1
      for (int s = 1; s < 4; ++s) {
        if (s < NF) accF += STEP; else tick(8);
        LST(s);
        dma<8, 65536, 1024>(sQ, qvo, qvo, qso + s * 8 * 65536, lds0 + s * 8192);
      }
    }
    const int kvbase = g * (SK * DH * 2) + sp * 32768;
    const int rot = b >> 3;
    int lb = lds0 + BUF;
    auto next_buf = [&]() { lb = lb == lds0 + 2 * BUF ? lds0 : lb + BUF; };
    {
      const v4u sK = mksrd(kc, 4 * SK * DH * 2);
#pragma unroll 1
      for (int c = 0; c < 8; ++c) {
#pragma unroll
        for (int u = 0; u < 4; ++u) {
          const int vo = ((u + rot) & 3) * 1024 + q4 * 256 + ((n ^ (4 * u + q4)) * 16);
          tick(8);
          if (c < 2) LST(4 + 4 * c + u);
          dma<8, 4096, 4096>(sK, vo, vo, kvbase + c * (64 * 32768), lb + u * 1024);
        }
        waitvm<32>(); LBAR(c);
        if (c == 0) STAMP(1);
        next_buf();
      }
    }
    STAMP(2);
    {
      const v4u sO = mksrd(wo, 4096 * D * 2);
      const int vo = ((lg >> 2) * 128 + (lg & 3)) * 2048 + wsw, so = (1024 * g1 + 8 * rq) * 2048 + 512 * c1;
#pragma unroll 1
      for (int k = 0; k < 4; ++k) {
        tick(8);
        dma<4, 128, 8192>(sO, vo, vo, so + k * 256 * 2048, lb + k * 1024);
        dma<4, 128, 8192>(sO, vo, vo, so + (k * 256 + 4) * 2048, lb + (k + 4) * 1024);
      }
      waitvm<32>(); LBAR(8);
      next_buf();
    }
    const v4u sG = mksrd(c2 < 8 ? wg : wu, D * F * 2), sD = mksrd(wd, F * D * 2);
    const int gu_vo = lg * (F * 2) + wsw, gu_so = 64 * r2 * (F * 2) + 896 * (c2 & 7);
    const int dn_vo = lg * 2048 + wsw, dn_so = 56 * r1 * 2048 + 512 * c1;
#pragma unroll 1
    for (int k = 0; k < 8; k += 2) {
      tick(8);
      dma<4, 128, 8192>(sG, gu_vo, gu_vo, gu_so + k * 8 * (F * 2), lb + k * 1024);
      dma<4, 128, 8192>(sG, gu_vo, gu_vo, gu_so + (k + 1) * 8 * (F * 2), lb + (k + 1) * 1024);
    }
    waitvm<32>(); bar();
    next_buf();
    if (PACE && TIMED) {
      const int d = __builtin_amdgcn_readfirstlane(*(LAS int*)(L + L_T + 4) - (int)tstart);
      tcommon = tstart + d;
      step += d * 256 / 32;
      accF += d * 256 / 32;
      asm volatile("s_nop 4" : "+s"(step), "+s"(accF), "+s"(tcommon));
    }
    auto groups = [&](int lo, int hi) {
      if (ABL & 16) return;
#pragma unroll
      for (int y = lo; y < hi; ++y) {
        if (y < 8) dma<3, 128, 8192>(sG, gu_vo, gu_vo, gu_so + y * 8 * (F * 2) + 512, lds0 + B_GUB + y * 1024);
        else dma<4, 128, 8192>(sD, dn_vo, dn_vo, dn_so + (y - 8) * 8 * 2048, lds0 + B_DN + (y - 8) * 1024);
      }
    };
    {
      const v4u sV = mksrd(vc, 4 * SK * DH * 2);
      const int va = q4 * 256 + ((n ^ (2 * q4)) * 16), vbb = q4 * 256 + ((n ^ (2 * q4 + 8)) * 16);
#pragma unroll 1
      for (int c = 0; c < 8; ++c) {
        if (TLBW && c == 6) dma_x1(sD, lane < 2 ? dn_so + lane * 55 * 2048 : F * D * 2, lds0 + L_JUNK);
        if (c < NB) {
          tick(3);
          dma<3, 128, 8192>(sG, gu_vo, gu_vo, gu_so + c * 8 * (F * 2) + 512, lds0 + B_GUB + c * 1024);
        }
#pragma unroll
        for (int u = 0; u < 4; ++u) {
          tick(8);
          dma<8, 4096, 4096>(sV, va, vbb, kvbase + c * (64 * 32768) + ((u + rot) & 3) * 1024, lb + u * 1024);
        }
        if (c == 7) STAMP(3);
        waitvm<32>(); LBAR(9 + c);
        next_buf();
      }
    }
    PAD(b, 0, 1);
    if (G1A > NB) tick(8);
    groups(NB, G1A);
    waitvm<16 + npieces(NB, G1A)>(); bar();
    if (G1B > G1A) tick(8);
    groups(G1A, G1B);
    PAD(b, 1, 2);
    waitvm<npieces(NB, G1B)>(); bar();
    STAMP(4);
    groups(G1B, G1END);
    const unsigned ldue1 = rebase(tcommon + LASTV + C1, 8, RB1) + TA;
    if (PACE && TIMED && G2A > G1END) pace_wait(ldue1);
    groups(G1END, G2A);
    if (PACE && TIMED && G2B > G2A) pace_wait(ldue1 + TB);
    groups(G2A, G2B);
    PAD(b, 2, 3);
    waitvm<npieces(G1END, G2B)>(); STAMP(5); bar();
    const unsigned ldue2 = rebase(tcommon + LASTV + C1 + C2, 14, RB) + TA;
    groups(G2B, G2END);
    if (PACE && TIMED && G3A > G2END) pace_wait(ldue2);
    groups(G2END, G3A);
    if (NEED_G0) { waitvm<npieces(G2END, G3A)>(); bar(); }
    if (PACE && TIMED && G3B > G3A) pace_wait(ldue2 + TB);
    groups(G3A, G3B);
    PAD(b, 3, 4);
    waitvm<npieces(G2END, G3B)>(); STAMP(8); bar();
    groups(G3B, NG);
    if (NEED_H0) { waitvm<0>(); STAMP(11); bar(); }
    STAMP_OUT();
    asm volatile("s_endpgm" ::: "memory");
  }

  int tid = threadIdx.x;
  PIN(tid);
  const int lane = tid & 63, lg = lane >> 3, lc = lane & 7, q4 = lane >> 4, n = lane & 15;
  const int cw = w - 1, t = cw * 64 + lane;
  const rsrc_t rW = mk(ws, WS_BYTES);
  const int rho = 32 * (q4 >> 1) + 2 * n + (q4 & 1);
  const int tro = ((lane >> 2) & 3) * 128 + (((2 * q4 + ((lane >> 1) & 1)) ^ (4 * ((lane >> 3) & 1))) * 16) + (lane & 1) * 8;
  if (ABL & 32) {
  stc4(rW, WS_S + b * 8192 + t * 16, v4u{SENT, SENT, SENT, SENT});
  if (cw == 1) stc1(rW, WS_S + 256 * 8192 + b * 256 + lane * 4, SENT);
  } else if (!(ABL & 1)) {
    const int h24 = (r1 * 1024 + 256 * c1) * 4 + lane * 16, h3 = S_H3 + ((r2 * 2 + (c2 >> 3)) * F + 448 * (c2 & 7)) * 4;
    const int co = cw < 4 ? S_H1 + b * 4096 + t * 16 : cw == 4 ? S_H2 + h24 : cw == 5 ? S_H4 + h24 : t < 496 ? h3 + (t - 384) * 16 : S_RS + vb * 256 + (t - 496) * 16;
    stc4(rW, co, v4u{SENT, SENT, SENT, SENT});
    if (cw == 1) stc4(rW, lane < 4 ? S_ML + b * 64 + lane * 16 : lane < 12 ? S_MU + vb * 128 + (lane - 4) * 16 : lane < 20 && c2 < 8 ? S_SS + (c2 * 16 + r2) * 128 + (lane - 12) * 16 : OOB, v4u{SENT, SENT, SENT, SENT});
  }
  STAMP(1);
  bar();
  v2u ep = *(LAS v2u*)(L + L_E);
  const v4u xa = *(LAS v4u*)(L + L_X + lane * 16), xb = *(LAS v4u*)(L + L_X + 1024 + lane * 16);
  const unsigned xr = *(LAS unsigned*)(L + L_X + (256 * r0 + 32 * cw + 2 * n) * 2);
  const unsigned xhr = *(LAS unsigned*)(L + L_X + (64 * r2 + (rho & ~1)) * 2);
  v2u wqt[8];
#pragma unroll
  for (int m = 0; m < 8; ++m) wqt[m] = trd(L + cw * 4096 + m * 512 + tro);
  float sumx, xh = (q4 & 1) ? bfhi(xhr) : bflo(xhr);
  unsigned xin;
  {
    float xv[16];
#pragma unroll
    for (int j = 0; j < 4; ++j) { xv[2 * j] = bflo(xa[j]); xv[2 * j + 1] = bfhi(xa[j]); xv[8 + 2 * j] = bflo(xb[j]); xv[9 + 2 * j] = bfhi(xb[j]); }
    float s = 0.f;
#pragma unroll
    for (int j = 0; j < 4; ++j) s = dot2(xb[j], 0x3F803F80u, dot2(xa[j], 0x3F803F80u, s));
    sumx = sum64(s);
    const float mean = sumx * (1.f / D);
    float ss = 0.f;
#pragma unroll
    for (int j = 0; j < 16; ++j) ss += (xv[j] - mean) * (xv[j] - mean);
    const float var = sum64(ss) * (1.f / D);
    const float mb = rbf(mean), den = rbf(__builtin_amdgcn_sqrtf(rbf(rbf(var) + 1e-5f)));
    xin = cvtpk(rbf(bflo(xr) - mb) / den, rbf(bfhi(xr) - mb) / den);
  }
  PIN(ep); PIN(xin); PIN(xh); PIN(sumx);
  const unsigned long long tag = (((unsigned long long)__builtin_amdgcn_readfirstlane(ep[1]) << 32) | (unsigned)__builtin_amdgcn_readfirstlane(ep[0])) + 1ull;
  const v2u tagv = {(unsigned)tag, (unsigned)(tag >> 32)};
  {
    ZERO4(a);
    asm volatile("s_nop 1");
    *(LAS float*)(L + L_QRED + t * 4) = gv16f(a, xin, wqt);
  }
  STAMP(13);
  bar();
  v4u kb[8][4];
  const int kro = cw * 4096 + n * 256;
#define KRD(c) do { _Pragma("unroll") for (int u = 0; u < 4; ++u) { kb[c][u] = *(LAS v4u*)(L + (((c) + 1) % 3) * BUF + kro + (((4 * u + q4) ^ n) * 16)); } \
    _Pragma("unroll") for (int u = 0; u < 4; ++u) PIN(kb[c][u]); } while (0)
  KRD(0);
  waitvm<0>();
  if (cw == 0) {
    float s = 0.f;
#pragma unroll
    for (int i = 0; i < 8; ++i) s += *(LAS float*)(L + L_QRED + (i * 64 + lane) * 4);
    stc1(rW, WS_QP + ((g * 4 + r0) * 1024 + 64 * c0 + lane) * 4, asu(s));
    if (TIMED) stc1(rW, lane == 0 ? WS_T0 + b * 4 : OOB, *(LAS unsigned*)(L + L_T));
  }
  bar();
  KRD(1);
  if (cw == 0) {
    waitvm<0>();
    stc2(rW, lane == 0 ? WS_FLAG + b * 8 : OOB, tagv);
  }
  bar();
  KRD(2);
  bar();
  const int fo = WS_FLAG + (lane < 32 ? 32 * cw + lane : 64 * g + 16 * ((lane >> 1) & 3) + 2 * cw + (lane & 1)) * 8;
  v2u fl = ldc2(rW, fo);
  KRD(3);
  bar();
  KRD(4);
#pragma nounroll
  for (int s = 0; s < SPIN_CAP; ++s) {
    if (!__any(fl[0] != tagv[0] || fl[1] != tagv[1])) break;
    asm volatile("" ::: "memory");
    fl = ldc2(rW, fo);
  }
  FENCE();
  v2u qg[4];
#pragma unroll
  for (int r = 0; r < 4; ++r) qg[r] = ldc2(rW, WS_QP + ((g * 4 + r) * 1024 + 2 * t) * 4);
  bar();
  v4u t0s = {0u, 0u, 0u, 0u};
  if (TIMED && cw == 4) t0s = ldc4(rW, WS_T0 + lane * 16);
  KRD(5);
  *(LAS unsigned*)(L + L_QS + cw * 272 + lane * 4) =
      cvtpk((asf(qg[0][0]) + asf(qg[1][0])) + (asf(qg[2][0]) + asf(qg[3][0])), (asf(qg[0][1]) + asf(qg[1][1])) + (asf(qg[2][1]) + asf(qg[3][1])));
  if (b == 0 && cw == 0) stc2(rW, lane == 0 ? WS_E : OOB, tagv);
  bar();
  v4u qf[4];
#pragma unroll
  for (int u = 0; u < 4; ++u) qf[u] = *(LAS v4u*)(L + L_QS + (n & 7) * 272 + 64 * u + 16 * q4);
  f32x4 S[8];
#define QK(c) do { f32x4 a_ = {0.f, 0.f, 0.f, 0.f}; \
    _Pragma("unroll") for (int u = 0; u < 4; ++u) a_ = mfma(kb[c][u], qf[u], a_); \
    S[c] = a_; PIN(S[c]); } while (0)
  QK(0); QK(1); KRD(6); QK(2); QK(3); QK(4); QK(5); QK(6);
  if (TIMED && cw == 4) {
    const unsigned own = *(LAS unsigned*)(L + L_T);
    float s = 0.f;
#pragma unroll
    for (int i = 0; i < 4; ++i) s += (float)min(max((int)(t0s[i] - own), -300), 300);
    *(LAS unsigned*)(L + L_T + 4) = own + (int)floorf(sum64(s) * (1.f / 256));
  }
  bar();
  KRD(7); QK(7);
  {
    float m = S[0][0];
#pragma unroll
    for (int c = 0; c < 8; ++c)
#pragma unroll
      for (int r = 0; r < 4; ++r) m = fmaxf(m, S[c][r]);
    m = max45(m);
    if (lane < 8) *(LAS float*)(L + L_MAX + (lane * 8 + cw) * 4) = m;
  }
  STAMP(2);
  bar();
  float M, lsum = 0.f;
  v2u wo_t[16], gua[16];
  const int tile = (cw & 3) * 8192 + tro;
#pragma unroll
  for (int m = 0; m < 16; ++m) wo_t[m] = trd(L + B_WO + tile + m * 512);
  if (cw >= 4) {
    const v4u ones = {0x3F803F80u, 0x3F803F80u, 0x3F803F80u, 0x3F803F80u};
#pragma unroll
    for (int k = 0; k < 4; ++k) {
      const LAS unsigned char* p = L + B_WO + (cw - 4) * 8192 + k * 2048 + n * 128 + q4 * 16;
      const f32x4 s = mfma(*(LAS v4u*)(p + 64), ones, mfma(*(LAS v4u*)p, ones, f32x4{0.f, 0.f, 0.f, 0.f}));
      if (n == 0) *(LAS f32x4*)(L + L_RS + (cw - 4) * 256 + k * 64 + q4 * 16) = s;
    }
  }
  {
    const f32x4 m0 = *(LAS f32x4*)(L + L_MAX + (n & 7) * 32), m1 = *(LAS f32x4*)(L + L_MAX + (n & 7) * 32 + 16);
    const float mr = fmaxf(fmaxf(fmaxf(m0[0], m0[1]), fmaxf(m0[2], m0[3])), fmaxf(fmaxf(m1[0], m1[1]), fmaxf(m1[2], m1[3])));
    M = rbf(rbf(mr) * SCALE);
  }
  auto softmax = [&](int c) {
    const bool hi = n >= 8;
    float p[4];
    const unsigned a0 = cvtpk(hi ? S[c + 4][0] : S[c][0], hi ? S[c + 4][1] : S[c][1]), a1 = cvtpk(hi ? S[c + 4][2] : S[c][2], hi ? S[c + 4][3] : S[c][3]);
    const unsigned s0 = cvtpk(bflo(a0) * SCALE, bfhi(a0) * SCALE), s1 = cvtpk(bflo(a1) * SCALE, bfhi(a1) * SCALE);
    p[0] = __builtin_amdgcn_exp2f((bflo(s0) - M) * LOG2E); p[1] = __builtin_amdgcn_exp2f((bfhi(s0) - M) * LOG2E);
    p[2] = __builtin_amdgcn_exp2f((bflo(s1) - M) * LOG2E); p[3] = __builtin_amdgcn_exp2f((bfhi(s1) - M) * LOG2E);
    lsum += (p[0] + p[1]) + (p[2] + p[3]);
    *(LAS v2u*)(L + L_P + (((c + 4 * (n >> 3)) * 4 + q4) * 4 + (cw >> 1)) * 128 + (n & 7) * 16 + (cw & 1) * 8) = v2u{cvtpk(p[0], p[1]), cvtpk(p[2], p[3])};
  };
  softmax(0); softmax(1);
  bar();
#pragma unroll
  for (int m = 0; m < 16; ++m) gua[m] = trd(L + B_GUA + tile + m * 512);
  if (cw == 7) {
    const LAS float* r = (const LAS float*)(L + L_RS) + rho;
    stc1(rW, S_RS + vb * 256 + lane * 4, asu((r[0] + r[64]) + (r[128] + r[192])));
  }
  softmax(2); softmax(3);
  lsum += dpp<R8>(lsum); lsum = sum45(lsum);
  if (lane < 8) *(LAS float*)(L + L_SUM + (lane * 8 + cw) * 4) = lsum;
  STAMP(3);
  bar();
  f32x4 O = {0.f, 0.f, 0.f, 0.f};
  const int vro = 2 * q4 * 4096 + ((lane >> 2) & 3) * 256 + (((2 * cw + ((lane >> 1) & 1)) ^ (2 * ((lane >> 2) & 3) + 8 * (q4 & 1))) * 16) + (lane & 1) * 8;
  const int pro = L_P + q4 * 128 + (n & 7) * 16;
#define PVT(c, u) do { const v2u a0_ = trd(L + ((11 + (c)) % 3) * BUF + (u) * 1024 + vro), a1_ = trd(L + ((11 + (c)) % 3) * BUF + (u) * 1024 + 4096 + vro); \
    const v4u p_ = *(LAS v4u*)(L + pro + ((c) * 4 + (u)) * 512); \
    O = mfma(v4u{a0_[0], a0_[1], a1_[0], a1_[1]}, p_, O); } while (0)
#define PV(c) do { PVT(c, 0); PVT(c, 1); PVT(c, 2); PVT(c, 3); PIN(O); } while (0)
  float rsum = 0.f;
  if (cw == 0) {
    const f32x4 l0 = *(LAS f32x4*)(L + L_SUM + (n & 7) * 32), l1 = *(LAS f32x4*)(L + L_SUM + (n & 7) * 32 + 16);
    const float Ls = ((l0[0] + l0[1]) + (l0[2] + l0[3])) + ((l1[0] + l1[1]) + (l1[2] + l1[3]));
    stc2(rW, lane < 8 ? S_ML + (b * 8 + lane) * 8 : OOB, v2u{asu(M), asu(Ls)});
  }
  PV(0);
  bar();
  PV(1);
  if (cw == 5) PADRUN(a);
  if (cw == 6) PADRUN(b);
  bar();
  const int mlo = S_ML + 64 * g * 64 + t * 8;
  v2u ml = ldc2(rW, mlo);
  unsigned rx[4];
  const int rso = cw == 7 ? S_RS + 4 * r1 * 256 + lane * 4 : OOB;
  if (cw == 7) {
#pragma unroll
    for (int i = 0; i < 4; ++i) rx[i] = ldc1(rW, rso + i * 256);
  } else {
#pragma unroll
    for (int i = 0; i < 4; ++i) rx[i] = 0u;
  }
  PV(2);
  bar();
  {
    PV(3);
    settle1<4>(rx, rW, rso, 256);
    rsum = (asf(rx[0]) + asf(rx[1])) + (asf(rx[2]) + asf(rx[3]));
    PIN(rsum);
#pragma nounroll
    for (int s = 0; s < SPIN_CAP; ++s) {
      if (!__any(ml[0] == SENT || ml[1] == SENT)) break;
      asm volatile("" ::: "memory");
      ml = ldc2(rW, mlo);
    }
    const float ms = asf(ml[0]);
    const float mw = max45(fmaxf(ms, dpp<R8>(ms)));
    float lw = asf(ml[1]) * __builtin_amdgcn_exp2f((ms - mw) * LOG2E);
    lw += dpp<R8>(lw); lw = sum45(lw);
    if (lane < 8) *(LAS f32x2*)(L + L_ML + (lane * 8 + cw) * 8) = f32x2{mw, lw};
  }
  bar();
  float oscale;
  {
    f32x4 a[4];
#pragma unroll
    for (int i = 0; i < 4; ++i) a[i] = *(LAS f32x4*)(L + L_ML + (n & 7) * 64 + i * 16);
    float Mg = fmaxf(a[0][0], a[0][2]);
#pragma unroll
    for (int i = 1; i < 4; ++i) Mg = fmaxf(Mg, fmaxf(a[i][0], a[i][2]));
    float Lg = 0.f;
#pragma unroll
    for (int i = 0; i < 4; ++i) Lg += a[i][1] * __builtin_amdgcn_exp2f((a[i][0] - Mg) * LOG2E) + a[i][3] * __builtin_amdgcn_exp2f((a[i][2] - Mg) * LOG2E);
    oscale = __builtin_amdgcn_exp2f((M - Mg) * LOG2E) / Lg;
    PIN(oscale);
  }
  PV(4);
  bar();
  PV(5);
  bar();
  PV(6);
  bar();
  PVT(7, 0); PVT(7, 1); PIN(O);
  v4u pl2 = *(LAS v4u*)(L + pro + 30 * 512), pl3 = *(LAS v4u*)(L + pro + 31 * 512);
  int h1o = n < 8 ? S_H1 + b * 4096 + cw * 512 + (q4 * 8 + n) * 16 : OOB;
  PIN(pl2); PIN(pl3); PIN(h1o);
  const unsigned due1 = __builtin_amdgcn_readfirstlane(*(LAS unsigned*)(L + L_T + 4)) + LASTV + C1;
  float fill = 0.f;
  PAD(a, 0, 1);
  bar();
  {
    const v2u a0 = trd(L + (18 % 3) * BUF + 2048 + vro), a1 = trd(L + (18 % 3) * BUF + 2048 + 4096 + vro);
    const v2u a2 = trd(L + (18 % 3) * BUF + 3072 + vro), a3 = trd(L + (18 % 3) * BUF + 3072 + 4096 + vro);
    FILL(fill);
    O = mfma(v4u{a0[0], a0[1], a1[0], a1[1]}, pl2, O);
    O = mfma(v4u{a2[0], a2[1], a3[0], a3[1]}, pl3, O);
    stc4(rW, h1o, v4u{asu(O[0] * oscale), asu(O[1] * oscale), asu(O[2] * oscale), asu(O[3] * oscale)});
  }
  FENCE();
  STAMP(4);
#define DUE(k, lead, early) time_wait(rebase(due1 + ((k) > 1 ? C2 : 0) + ((k) > 2 ? C3 : 0) + ((k) > 3 ? C4 : 0), lead, (k) == 1 ? RB1 : RB) - (early))
  const int rdo = ((rho & 3) * 16 + (rho >> 2)) * 16;

  if (cw < 4) {
    __builtin_amdgcn_s_setprio(2);
    PAD(a, 1, 2);
    v2u gub[16], dnt[14];
    auto tiles = [&](int lo, int hi) {
#pragma unroll
      for (int y = lo; y < hi; ++y)
#pragma unroll
        for (int h = 0; h < 2; ++h) {
          if (y < 8) gub[2 * y + h] = trd(L + B_GUB + tile + (2 * y + h) * 512);
          else dnt[2 * (y - 8) + h] = trd(L + B_DN + tile + (2 * (y - 8) + h) * 512);
        }
      FENCE();
    };
    LAS float* rw = (LAS float*)(L + L_R1 + lane * 16 + cw * 4);
    LAS float* rw3 = (LAS float*)(L + L_R3 + lane * 16 + ((cw & 1) * 2 + (cw >> 1)) * 4);
    LAS f32x4* rr = (LAS f32x4*)(L + L_R1 + rdo);
    int h24o = (r1 * 1024 + 256 * c1 + t) * 4;
    int h3o = S_H3 + ((r2 * 2 + (c2 >> 3)) * F + 448 * (c2 & 7) + t) * 4;
    int g1o = S_H1 + (64 * g1 + 16 * cw + q4) * 4096 + rq * 256 + n * 16;
    PIN(rw); PIN(rw3); PIN(rr); PIN(h24o); PIN(h3o); PIN(g1o);
#define GLD(o, so) __builtin_amdgcn_raw_buffer_load_b128(rW, o, so, SC1)
    DUE(1, -2, 0);
    FENCE();
    {
      v4u v[4];
#pragma unroll
      for (int i = 0; i < 4; ++i) v[i] = GLD(g1o, i * 4 * 4096);
      FENCE();
      tiles(0, NB);
      ZERO4(a1);
      PAD(a, 2, 3);
      FENCE();
      float r = red4(((asf4(v[0]) + asf4(v[1])) + asf4(v[2])) + asf4(v[3]));
      if (__builtin_expect(__any(r != r), 0)) { PAD(a, 10, 11); RETRY(1); settle4<4>(v, rW, g1o, 4 * 4096); r = red4(((asf4(v[0]) + asf4(v[1])) + asf4(v[2])) + asf4(v[3])); }
      *rw = r;
      STAMP(5);
      FILL(fill);
      bar();
      const f32x4 p = *rr;
      FILL(fill);
      unsigned in0, in1;
      pack64((p[0] + p[2]) + (p[1] + p[3]), in0, in1);
      gv16(a1, in0, wo_t);
      __builtin_amdgcn_raw_buffer_store_b32(asu(gv16f(a1, in1, wo_t + 8)), rW, h24o, S_H2, SC1);
    }
    FENCE();
    STAMP(6);
    PAD(a, 3, 4);
    tiles(NB, G1END);
    int g2o = S_H2 + ((16 * cw + q4) * 1024 + 64 * r2) * 4 + n * 16;
    PIN(g2o);
    DUE(2, -4, 0);
    FENCE();
    {
      v4u v[4];
#pragma unroll
      for (int i = 0; i < 4; ++i) v[i] = GLD(g2o, i * 4 * 4096);
      FENCE();
      if (NEED_G0) { bar(); tiles(G1END, G2END); }
      ZERO4(a2); ZERO4(a3);
      PAD(a, 4, 5);
      FENCE();
      float r = red4(((asf4(v[0]) + asf4(v[1])) + asf4(v[2])) + asf4(v[3]));
      if (__builtin_expect(__any(r != r), 0)) { PAD(a, 11, 12); RETRY(2); settle4<4>(v, rW, g2o, 4 * 4096); r = red4(((asf4(v[0]) + asf4(v[1])) + asf4(v[2])) + asf4(v[3])); }
      rw[256] = r;
      STAMP(7);
      PAD(a, 5, 6);
      FILL(fill);
      bar();
      STAMP(8);
      const f32x4 p = rr[64];
      const float mu = *(LAS float*)(L + L_MU);
      FILL(fill);
      unsigned in0, in1;
      pack64(rbf(xh + rbf((p[0] + p[2]) + (p[1] + p[3]))) - mu, in0, in1);
      gv16(a2, in0, gua);
      stc1(rW, h3o, asu(gv16f(a2, in1, gua + 8)));
      PAD(a, 6, 7);
      if (cw < 3) {
        gv16(a3, in0, gub);
        stc1(rW, h3o + 1024, asu(gv16f(a3, in1, gub + 8)));
      }
    }
    FENCE();
    STAMP(9);
    PAD(a, 7, 8);
    if (!NEED_G0) tiles(G1END, G2END);
    int g3o = n < 14 ? S_H3 + (((8 * (cw & 1) + q4) * 2 + (cw >> 1)) * F + 56 * r1) * 4 + n * 16 : OOB;
    PIN(g3o);
    DUE(3, -4, 0);
    FENCE();
    {
      v4u v[2];
#pragma unroll
      for (int i = 0; i < 2; ++i) v[i] = GLD(g3o, i * 8 * F * 4);
      FENCE();
      if (NEED_H0) { bar(); tiles(G2END, NG); }
      ZERO4(a4);
      PAD(a, 8, 9);
      FENCE();
      float r = red4(asf4(v[0]) + asf4(v[1]));
      if (__builtin_expect(__any(r != r), 0)) { PAD(a, 12, e); RETRY(3); settle4<2>(v, rW, g3o, 8 * F * 4); r = red4(asf4(v[0]) + asf4(v[1])); }
      *rw3 = r;
      STAMP(10);
      PAD(a, 9, 10);
      FILL(fill);
      bar();
      STAMP(11);
      const f32x4 p = rr[128];
      const float rden = *(LAS float*)(L + L_MU + 4);
      FILL(fill);
      const unsigned gu = cvtpk((p[0] + p[2]) * rden, (p[1] + p[3]) * rden);
      const float gv = bflo(gu), uv = bfhi(gu);
      const float z2 = gv * __builtin_fmaf(gv * gv, -2.f * 0.7978845608028654f * LOG2E * 0.044715f, -2.f * 0.7978845608028654f * LOG2E);
      const float ge = rbf(gv * __builtin_amdgcn_rcpf(1.f + __builtin_amdgcn_exp2f(z2)));
      unsigned in0, in1;
      pack64(ge * uv, in0, in1);
      gv16(a4, in0, dnt);
      __builtin_amdgcn_raw_buffer_store_b32(asu(gv12f(a4, in1, dnt + 8)), rW, h24o, S_H4, SC1);
    }
    FENCE();
    STAMP(12);
    STAMP(15);
    STAMP_OUT();
    asm volatile("s_endpgm" ::: "memory");
  }
  if (cw == 4) {
    PAD(b, 4, 5);
    int muo = S_MU + (4 * lane + (r2 >> 2)) * 128, sso = S_SS + ((vb >> 5) * 16 + n) * 128;
    PIN(muo); PIN(sso);
    bar();
    DUE(2, 14, EM);
    FENCE();
    {
      unsigned m[1];
      m[0] = ldc1(rW, muo);
      if (NEED_G0) bar();
      if (__builtin_expect(__any(m[0] == SENT), 0)) { PAD(b, 9, 10); RETRY(2); settle1<1>(m, rW, muo, 0); }
      const float ts = sum64(asf(m[0]));
      *(LAS float*)(L + L_MU) = (sumx + ts) * (1.f / D);
    }
    STAMP(7);
    bar();
    DUE(3, 20, ES);
    FENCE();
    {
      unsigned s[1];
      s[0] = ldc1(rW, sso);
      if (NEED_H0) bar();
      PAD(b, 5, 6);
      if (__builtin_expect(__any(s[0] == SENT), 0)) { PAD(b, 10, 11); RETRY(3); settle1<1>(s, rW, sso, 0); }
      const float var = sum16(asf(s[0])) * (1.f / D);
      *(LAS float*)(L + L_MU + 4) = __builtin_amdgcn_rcpf(rbf(__builtin_amdgcn_sqrtf(rbf(rbf(var) + 1e-5f))));
    }
    STAMP(10);
    bar();
    STAMP(15);
    STAMP_OUT();
    asm volatile("s_endpgm" ::: "memory");
  }
  if (cw == 7) {
    PAD(b, 6, 7);
    bar();
    {
      const f32x4 p = *(LAS f32x4*)(L + L_R1 + rdo);
      const float tj = sum64(rbf((p[0] + p[2]) + (p[1] + p[3])) * rsum);
      stc1(rW, lane == 0 ? S_MU + vb * 128 : OOB, asu(tj));
    }
    STAMP(6);
    if (NEED_G0) bar();
    PAD(b, 7, 8);
    bar();
    float hfin;
    {
      const f32x4 p = *(LAS f32x4*)(L + L_R2 + rdo);
      const float mu = *(LAS float*)(L + L_MU);
      const float h = rbf(xh + rbf((p[0] + p[2]) + (p[1] + p[3]))), d = h - mu;
      const float ss = sum64(d * d);
      stc1(rW, lane == 0 && c2 < 8 ? S_SS + (c2 * 16 + r2) * 128 : OOB, asu(ss));
      const int e = 4 * c2 + q4;
      hfin = __shfl(h, 32 * (e >> 5) + 16 * (e & 1) + ((e >> 1) & 15));
      PIN(hfin);
    }
    STAMP(9);
    if (NEED_H0) bar();
    PAD(b, 8, 9);
    bar();
    const int g4o = S_H4 + lane * 4096 + vb * 16;
    DUE(4, 16, 0);
    FENCE();
    v4u v[1];
    v[0] = ldc4(rW, g4o);
    int oo = n == 0 ? (4 * vb + q4) * 2 : D * 2;
    PIN(oo);
    FENCE();
    float y = red4(asf4(v[0]));
    if (__builtin_expect(__any(y != y), 0)) { asm volatile(".Lpf_end:"); PAD(b, 11, e); RETRY(1); settle4<1>(v, rW, g4o, 0); y = red4(asf4(v[0])); }
    STAMP(13);
    y = sum16(y);
    __builtin_amdgcn_raw_buffer_store_b16((u16)cvtpk(hfin + rbf(y), 0.f), mk(out, D * 2), oo, 0, OUTSC);
    STAMP(14);
  }
  STAMP(15);
  STAMP_OUT();
}

extern "C" void kernel_launch(void* const* d_in, const int* in_sizes, int n_in, void* d_out, int out_size, void* d_ws,
                              size_t ws_size, hipStream_t stream) {
  fused_decode_block<<<256, 576, 0, stream>>>((unsigned char*)d_ws, (const u16*)d_in[3], (const u16*)d_in[0], (const u16*)d_in[1],
                                              (const u16*)d_in[2], (const u16*)d_in[4], (const u16*)d_in[6], (const u16*)d_in[7],
                                              (const u16*)d_in[8], (u16*)d_out TUNEVAL);
}
```
